```python
import torch

def _softmax(x, axis=-1):
    return torch.softmax(x, dim=axis)
def _gelu(x):
    return torch.nn.functional.gelu(x, approximate="tanh")
def _silu(x):
    return torch.nn.functional.silu(x)
def _sigmoid(x):
    return torch.sigmoid(x)
def _layernorm(x, axis=-1, eps=1e-5):
    m = x.mean(dim=axis, keepdim=True)
    v = x.var(dim=axis, keepdim=True, unbiased=False)
    return (x - m) / torch.sqrt(v + eps)
def _scatter_add(tgt, idx, src, axis=0):
    return tgt.clone().index_add_(axis, idx, src)
def _segment_sum(src, seg, n):
    out = torch.zeros((n,) + src.shape[1:], dtype=src.dtype)
    return out.index_add_(0, seg, src)
def _pad(x, pw):
    flat = tuple(v for pair in reversed(pw) for v in pair)
    return torch.nn.functional.pad(x, flat)

kernel_name = 'parallel_sum__ssm_conv_s4__mixer_channel_mlp__prompt'

def setup_inputs():

    _w = (lambda a: (lambda t: t.to(torch.bfloat16) if t.is_floating_point() else t)(torch.as_tensor(a)))
    def _A_setup():
        B, L, D, N = 1, 2048, 512, 128
        A_re = -(lambda _x: torch.exp(torch.as_tensor(_x)))((lambda *s: torch.randn(*s, dtype=torch.bfloat16) * 3.2)(D, N))
        A_im = torch.pi * (lambda *s: torch.randn(*s, dtype=torch.bfloat16) * 0.05)(D, N)
        B_re = (lambda *s: torch.randn(*s, dtype=torch.bfloat16) * 0.1)(D, N)
        B_im = (lambda *s: torch.randn(*s, dtype=torch.bfloat16) * 0.1)(D, N)
        C_re = (lambda *s: torch.randn(*s, dtype=torch.bfloat16) * 0.1)(D, N)
        C_im = (lambda *s: torch.randn(*s, dtype=torch.bfloat16) * 0.1)(D, N)
        log_dt = (lambda *s: torch.randn(*s, dtype=torch.bfloat16) * 0.05)(D) * 0.5 - 4.0
        Dskip = (lambda *s: torch.randn(*s, dtype=torch.bfloat16) * 0.05)(D)
        return {
            "u": _w((lambda *s: torch.randn(*s, dtype=torch.bfloat16) * 0.05)(B, L, D)),
            "A_re": _w(A_re),
            "A_im": _w(A_im),
            "B_re": _w(B_re),
            "B_im": _w(B_im),
            "C_re": _w(C_re),
            "C_im": _w(C_im),
            "log_dt": _w(log_dt),
            "Dskip": _w(Dskip),
        }
    _A_in = _A_setup()

    def _B_setup():
        B, S, D, F = 1, 2048, 512, 1792
        return {
            "x": _w((lambda *s: torch.randn(*s, dtype=torch.bfloat16) * 0.05)(B, S, D)),
            "w1": _w((lambda *s: torch.randn(*s, dtype=torch.bfloat16) * 1.6)(D, F) * (D ** -0.5)),
            "b1": _w((lambda *s: torch.randn(*s, dtype=torch.bfloat16) * 0.8)(F)),
            "w2": _w((lambda *s: torch.randn(*s, dtype=torch.bfloat16) * 0.05)(F, D) * (F ** -0.5)),
            "b2": _w((lambda *s: torch.randn(*s, dtype=torch.bfloat16) * 0.05)(D)),
        }
    _B_in = _B_setup()

    return {
        "A_u": _A_in["u"],
        "A_A_re": _A_in["A_re"],
        "A_A_im": _A_in["A_im"],
        "A_B_re": _A_in["B_re"],
        "A_B_im": _A_in["B_im"],
        "A_C_re": _A_in["C_re"],
        "A_C_im": _A_in["C_im"],
        "A_log_dt": _A_in["log_dt"],
        "A_Dskip": _A_in["Dskip"],
        "B_x": _B_in["x"],
        "B_w1": _B_in["w1"],
        "B_b1": _B_in["b1"],
        "B_w2": _B_in["w2"],
        "B_b2": _B_in["b2"],
    }

def reference(A_u, A_A_re, A_A_im, A_B_re, A_B_im, A_C_re, A_C_im, A_log_dt, A_Dskip, B_x, B_w1, B_b1, B_w2, B_b2):
    def _A_ref(u, A_re, A_im, B_re, B_im, C_re, C_im, log_dt, Dskip):
        Bsz, L, D = u.shape
        dt = torch.exp(log_dt)[:, None]
        A = A_re + 1j * A_im
        Bc = B_re + 1j * B_im
        Cc = C_re + 1j * C_im
        logdA = dt * A
        dB = dt * Bc
        l = (lambda x, dt: x.to(dt))(torch.arange(L), A_re.dtype)
        P = torch.exp(logdA[:, :, None] * l[None, None, :])
        K = torch.einsum("dn,dnl,dn->dl", Cc, P, dB).real
        L2 = 2 * L
        Kp = _pad(K, ((0, 0), (0, L)))
        up = (lambda x, *d: x.permute(*d))(u, 0, 2, 1)
        up = _pad(up, ((0, 0), (0, 0), (0, L)))
        Kf = (lambda x, axes=(-2,-1): torch.fft.rfft2(x.float(), dim=axes))(Kp[:, None, :], axes=(-2, -1))
        Uf = (lambda x, axes=(-2,-1): torch.fft.rfft2(x.float(), dim=axes))(up[:, :, None, :], axes=(-2, -1))
        Yf = Uf * Kf[None, :, :, :]
        y = (lambda x, s, axes=(-2,-1): torch.fft.irfft2(x, s=s, dim=axes).to(torch.bfloat16))(Yf, (1, L2), axes=(-2, -1))[:, :, 0, :L]
        y = (lambda x, *d: x.permute(*d))(y, 0, 2, 1) + u * Dskip[None, None, :]
        return y

    def _B_ref(x, w1, b1, w2, b2):
        y = _layernorm(x, axis=-1)
        h = _gelu(torch.einsum("bsd,df->bsf", y, w1) + b1)
        return torch.einsum("bsf,fd->bsd", h, w2) + b2

    x = A_u
    xn = _layernorm(x, axis=-1)
    return x + _A_ref(xn, A_A_re, A_A_im, A_B_re, A_B_im, A_C_re, A_C_im, A_log_dt, A_Dskip) + _B_ref(xn, B_w1, B_b1, B_w2, B_b2)
```

```cpp
#include <hip/hip_runtime.h>
#include <hip/hip_bf16.h>
#include <stdint.h>

typedef __attribute__((ext_vector_type(8))) short bf16x8;
typedef __attribute__((ext_vector_type(4))) float f32x4;
typedef __attribute__((ext_vector_type(16))) float f32x16;
typedef unsigned short u16;

#define L_ 2048
#define D_ 512
#define N_ 128
#define F_ 1792

__device__ __forceinline__ float bf2f(u16 v) { return __uint_as_float(((unsigned)v) << 16); }
__device__ __forceinline__ u16 f2bf(float f) {
  unsigned u = __float_as_uint(f);
  u += 0x7FFFu + ((u >> 16) & 1u);
  return (u16)(u >> 16);
}
__device__ __forceinline__ float rbf(float f) { return bf2f(f2bf(f)); }

__device__ __forceinline__ float wsum(float v) {
#pragma unroll
  for (int o = 32; o > 0; o >>= 1) v += __shfl_xor(v, o);
  return v;
}

#define LN_BLOCKS 128
#define TR_BLOCKS 448
__device__ void ln_rows(const u16* __restrict__ x, u16* __restrict__ xn, u16* __restrict__ y2,
                        u16* __restrict__ xnT, int blk, u16* sT) {
  int wave = threadIdx.x >> 6, lane = threadIdx.x & 63;
  for (int rr = 0; rr < 4; ++rr) {
    int rloc = wave * 4 + rr;
    int row = blk * 16 + rloc;
    bf16x8 v = *(const bf16x8*)(x + (size_t)row * D_ + lane * 8);
    float f[8];
#pragma unroll
    for (int pass = 0; pass < 2; ++pass) {
      float s = 0.f;
#pragma unroll
      for (int e = 0; e < 8; ++e) { f[e] = bf2f((u16)v[e]); s += f[e]; }
      float mean = wsum(s) * (1.f / D_);
      float q = 0.f;
#pragma unroll
      for (int e = 0; e < 8; ++e) { float dd = f[e] - mean; q += dd * dd; }
      float var = rbf(wsum(q) * (1.f / D_));
      float mb = rbf(mean);
      float den = rbf(sqrtf(rbf(var + 1e-5f)));
#pragma unroll
      for (int e = 0; e < 8; ++e) v[e] = (short)f2bf(rbf(f[e] - mb) / den);
      if (pass == 0) {
        *(bf16x8*)(xn + (size_t)row * D_ + lane * 8) = v;
#pragma unroll
        for (int e = 0; e < 8; ++e) sT[(lane * 8 + e) * 16 + rloc] = (u16)v[e];
      } else {
        *(bf16x8*)(y2 + (size_t)row * D_ + lane * 8) = v;
      }
    }
  }
  __syncthreads();
  for (int idx = threadIdx.x; idx < D_ * 2; idx += 256) {
    int d = idx >> 1, h = idx & 1;
    *(bf16x8*)(xnT + (size_t)d * L_ + blk * 16 + h * 8) = *(const bf16x8*)(sT + d * 16 + h * 8);
  }
}

__device__ void tr_tile(const u16* __restrict__ src, u16* __restrict__ dst, int R, int C, int tr, int tc, u16* s) {
  for (int idx = threadIdx.x; idx < 64 * 64; idx += 256) {
    int r = idx >> 6, c = idx & 63;
    s[c * 65 + r] = src[(size_t)(tr * 64 + r) * C + tc * 64 + c];
  }
  __syncthreads();
  for (int idx = threadIdx.x; idx < 64 * 64; idx += 256) {
    int c = idx >> 6, r = idx & 63;
    dst[(size_t)(tc * 64 + c) * R + tr * 64 + r] = s[c * 65 + r];
  }
}

__device__ __forceinline__ void cexp(float ar, float ai, float l, float& er, float& ei) {
  float m = __expf(ar * l);
  float r = ai * l * 0.15915494309189535f;
  r -= floorf(r);
  er = m * __builtin_amdgcn_cosf(r);
  ei = m * __builtin_amdgcn_sinf(r);
}

#define AS 260
__device__ void kgen(int d, const u16* Are, const u16* Aim, const u16* Bre, const u16* Bim,
                     const u16* Cre, const u16* Cim, const u16* logdt,
                     u16* __restrict__ Khi, u16* __restrict__ Klo, float* sm) {
  float* sA = sm;
  float* sB = sA + 41 * AS;
  float* sP = sB + 4 * 256 * 16;
  float* sK = sP + 512;
  int t = threadIdx.x;
  if (t < N_) {
    float dt = rbf(__expf(bf2f(logdt[d])));
    int o = d * N_ + t;
    float br = dt * bf2f(Bre[o]), bi = dt * bf2f(Bim[o]);
    float cr = bf2f(Cre[o]), ci = bf2f(Cim[o]);
    sP[t] = dt * bf2f(Are[o]);
    sP[128 + t] = dt * bf2f(Aim[o]);
    sP[256 + t] = cr * br - ci * bi;
    sP[384 + t] = cr * bi + ci * br;
  }
  __syncthreads();
  {
    int n = t & 127;
    float ar = sP[n], ai = sP[128 + n], cbr = sP[256 + n], cbi = sP[384 + n];
    for (int row = (t >> 7); row < 41; row += 2) {
      int r, j;
      if (row < 17) { r = 0; j = row; } else { r = 1 + (row - 17) / 8; j = (row - 17) & 7; }
      float base = (r == 0) ? 0.f : (float)(128 << r);
      float s = (float)(1 << r);
      float er, ei; cexp(ar, ai, base + 16.f * s * j, er, ei);
      sA[row * AS + n] = cbr * er - cbi * ei;
      sA[row * AS + 128 + n] = -(cbr * ei + cbi * er);
    }
    for (int ri = (t >> 7); ri < 64; ri += 2) {
      int r = ri >> 4, i = ri & 15;
      float er, ei; cexp(ar, ai, (float)((1 << r) * i), er, ei);
      sB[(r * 256 + n) * 16 + i] = er;
      sB[(r * 256 + 128 + n) * 16 + i] = ei;
    }
  }
  __syncthreads();
  int wave = t >> 6, lane = t & 63;
  for (int tile = wave; tile < 5; tile += 4) {
    int r = (tile < 2) ? 0 : tile - 1;
    int row0 = (tile < 2) ? tile * 16 : 17 + (tile - 2) * 8;
    int nrow = (tile == 0) ? 16 : (tile == 1 ? 1 : 8);
    int rr = lane & 15; if (rr >= nrow) rr = 0;
    const float* pa = sA + (row0 + rr) * AS + (lane >> 4);
    const float* pb = sB + r * 4096 + (lane >> 4) * 16 + (lane & 15);
    f32x4 acc = {0, 0, 0, 0};
#pragma unroll 8
    for (int kk = 0; kk < 64; ++kk)
      acc = __builtin_amdgcn_mfma_f32_16x16x4f32(pa[kk * 4], pb[kk * 64], acc, 0, 0, 0);
#pragma unroll
    for (int v = 0; v < 4; ++v) sK[tile * 256 + ((lane >> 4) * 4 + v) * 16 + (lane & 15)] = acc[v];
  }
  __syncthreads();
  for (int l = t; l < L_; l += 256) {
    int lb = (int)bf2f(f2bf((float)l));
    int idx;
    if (lb <= 256) idx = lb;
    else {
      int r = (lb <= 512) ? 1 : (lb <= 1024 ? 2 : 3);
      int q = (lb - (128 << r)) >> r;
      if (q == 128) { r += 1; q = 0; if (r == 4) { r = 3; q = 128; } }
      idx = (r == 3 && q == 128) ? -1 : (512 + (r - 1) * 256 + q);
    }
    float k;
    if (lb == 2048) {
      float acc = 0.f;
      for (int n = 0; n < N_; ++n) {
        float er, ei; cexp(sP[n], sP[128 + n], 2048.f, er, ei);
        acc += sP[256 + n] * er - sP[384 + n] * ei;
      }
      k = acc;
    } else k = sK[idx];
    u16 hi = f2bf(k);
    Khi[(size_t)d * L_ + l] = hi;
    Klo[(size_t)d * L_ + l] = f2bf(k - bf2f(hi));
  }
}

__global__ __launch_bounds__(256) void k1_prep(const u16* x, const u16* Are, const u16* Aim, const u16* Bre,
    const u16* Bim, const u16* Cre, const u16* Cim, const u16* logdt, const u16* w1, const u16* w2,
    u16* xn, u16* y2, u16* xnT, u16* w1T, u16* w2T, u16* Khi, u16* Klo) {
  extern __shared__ float smem[];
  int b = blockIdx.x;
  if (b < D_) { kgen(b, Are, Aim, Bre, Bim, Cre, Cim, logdt, Khi, Klo, smem); return; }
  b -= D_;
  if (b < LN_BLOCKS) { ln_rows(x, xn, y2, xnT, b, (u16*)smem); return; }
  b -= LN_BLOCKS;
  if (b < 224) tr_tile(w1, w1T, D_, F_, b / 28, b % 28, (u16*)smem);
  else { b -= 224; tr_tile(w2, w2T, F_, D_, b / 8, b % 8, (u16*)smem); }
}

#define LDT 40
template <int BM, int BN>
__device__ __forceinline__ void gemm_core(const u16* __restrict__ A, const u16* __restrict__ Bt, int K,
                                          int m0, int n0, u16* sm, f32x16 (&acc)[BM / 64][BN / 64]) {
  constexpr int TM = BM / 64, TN = BN / 64;
  int t = threadIdx.x, wave = t >> 6, lane = t & 63;
  int wm = (wave >> 1) * (BM / 2), wn = (wave & 1) * (BN / 2);
  u16* sA = sm; u16* sB = sm + 2 * BM * LDT;
#pragma unroll
  for (int i = 0; i < TM; ++i)
#pragma unroll
    for (int j = 0; j < TN; ++j)
#pragma unroll
      for (int v = 0; v < 16; ++v) acc[i][j][v] = 0.f;
  constexpr int NA = BM * 4 / 256, NB = BN * 4 / 256;
  bf16x8 ra[NA], rb[NB];
  auto gload = [&](int k0) {
#pragma unroll
    for (int i = 0; i < NA; ++i) { int c = t + i * 256; ra[i] = *(const bf16x8*)(A + (size_t)(m0 + (c >> 2)) * K + k0 + (c & 3) * 8); }
#pragma unroll
    for (int i = 0; i < NB; ++i) { int c = t + i * 256; rb[i] = *(const bf16x8*)(Bt + (size_t)(n0 + (c >> 2)) * K + k0 + (c & 3) * 8); }
  };
  auto swrite = [&](int buf) {
#pragma unroll
    for (int i = 0; i < NA; ++i) { int c = t + i * 256; *(bf16x8*)(sA + buf * BM * LDT + (c >> 2) * LDT + (c & 3) * 8) = ra[i]; }
#pragma unroll
    for (int i = 0; i < NB; ++i) { int c = t + i * 256; *(bf16x8*)(sB + buf * BN * LDT + (c >> 2) * LDT + (c & 3) * 8) = rb[i]; }
  };
  gload(0); swrite(0); __syncthreads();
  int nk = K / 32;
  for (int kt = 0; kt < nk; ++kt) {
    int buf = kt & 1;
    if (kt + 1 < nk) gload((kt + 1) * 32);
#pragma unroll
    for (int ks = 0; ks < 2; ++ks) {
      bf16x8 fa[TM], fb[TN];
#pragma unroll
      for (int i = 0; i < TM; ++i) fa[i] = *(const bf16x8*)(sA + buf * BM * LDT + (wm + i * 32 + (lane & 31)) * LDT + ks * 16 + (lane >> 5) * 8);
#pragma unroll
      for (int j = 0; j < TN; ++j) fb[j] = *(const bf16x8*)(sB + buf * BN * LDT + (wn + j * 32 + (lane & 31)) * LDT + ks * 16 + (lane >> 5) * 8);
#pragma unroll
      for (int i = 0; i < TM; ++i)
#pragma unroll
        for (int j = 0; j < TN; ++j) acc[i][j] = __builtin_amdgcn_mfma_f32_32x32x16_bf16(fa[i], fb[j], acc[i][j], 0, 0, 0);
    }
    if (kt + 1 < nk) swrite(buf ^ 1);
    __syncthreads();
  }
}

__device__ __forceinline__ float gelu_t(float x) {
  float u = 0.7978845608028654f * (x + 0.044715f * x * x * x);
  return 0.5f * x * (1.f + tanhf(u));
}

#define RLEN 2112
__device__ void conv_ch(int d, const u16* __restrict__ Khi, const u16* __restrict__ Klo,
                        const u16* __restrict__ xnT, u16* __restrict__ convT, u16* sm) {
  u16* sR = sm;
  u16* sU = sm + 16 * RLEN;
  float* sRed = (float*)sR;
  int t = threadIdx.x, wave = t >> 6, lane = t & 63;
  for (int y = t; y < RLEN; y += 256) {
#pragma unroll
    for (int c = 0; c < 8; ++c) {
      int xx = y + c;
      u16 h = 0, lo = 0;
      if (xx < L_) { h = Khi[(size_t)d * L_ + (L_ - 1 - xx)]; lo = Klo[(size_t)d * L_ + (L_ - 1 - xx)]; }
      sR[c * RLEN + y] = h; sR[(8 + c) * RLEN + y] = lo;
    }
  }
  for (int i = t; i < 128; i += 256) *(bf16x8*)(sU + i * 8) = (bf16x8){0, 0, 0, 0, 0, 0, 0, 0};
  *(bf16x8*)(sU + 1024 + t * 8) = *(const bf16x8*)(xnT + (size_t)d * L_ + t * 8);
  __syncthreads();
  int q16 = lane & 15, g = lane >> 4;
  int c = (7 - q16) & 7;
  const u16* pR = sR + c * RLEN + (2047 - q16 + 8 * g - c);
  f32x4 acc[4][2];
#pragma unroll
  for (int a = 0; a < 4; ++a) { acc[a][0] = (f32x4){0, 0, 0, 0}; acc[a][1] = (f32x4){0, 0, 0, 0}; }
  for (int m = wave; m < 32; m += 4) {
#pragma unroll
    for (int sc = 0; sc < 2; ++sc) {
      bf16x8 b1 = *(const bf16x8*)(sU + 1024 + 64 * (16 + q16 - m) + 32 * sc + 8 * g);
      bf16x8 b0 = (bf16x8){0, 0, 0, 0, 0, 0, 0, 0};
      if (m < 16) b0 = *(const bf16x8*)(sU + 1024 + 64 * (q16 - m) + 32 * sc + 8 * g);
#pragma unroll
      for (int qt = 0; qt < 4; ++qt) {
        int tt = 4 * m + qt - 2 * sc;
        bf16x8 ah = *(const bf16x8*)(pR - 16 * tt);
        bf16x8 al = *(const bf16x8*)(pR + 8 * RLEN - 16 * tt);
        acc[qt][1] = __builtin_amdgcn_mfma_f32_16x16x32_bf16(ah, b1, acc[qt][1], 0, 0, 0);
        acc[qt][1] = __builtin_amdgcn_mfma_f32_16x16x32_bf16(al, b1, acc[qt][1], 0, 0, 0);
        if (m < 16) {
          acc[qt][0] = __builtin_amdgcn_mfma_f32_16x16x32_bf16(ah, b0, acc[qt][0], 0, 0, 0);
          acc[qt][0] = __builtin_amdgcn_mfma_f32_16x16x32_bf16(al, b0, acc[qt][0], 0, 0, 0);
        }
      }
    }
  }
  __syncthreads();
#pragma unroll
  for (int qt = 0; qt < 4; ++qt)
#pragma unroll
    for (int pt = 0; pt < 2; ++pt)
#pragma unroll
      for (int v = 0; v < 4; ++v)
        sRed[wave * L_ + 64 * (pt * 16 + q16) + 16 * qt + 4 * g + v] = acc[qt][pt][v];
  __syncthreads();
  for (int l = t * 8; l < L_; l += 2048) {
    bf16x8 o;
#pragma unroll
    for (int e = 0; e < 8; ++e)
      o[e] = (short)f2bf(sRed[l + e] + sRed[L_ + l + e] + sRed[2 * L_ + l + e] + sRed[3 * L_ + l + e]);
    *(bf16x8*)(convT + (size_t)d * L_ + l) = o;
  }
}

__global__ __launch_bounds__(256) void k2_gemm1_conv(const u16* y2, const u16* w1T, const u16* b1, u16* h,
    const u16* Khi, const u16* Klo, const u16* xnT, u16* convT) {
  extern __shared__ float smem[];
  int b = blockIdx.x;
  if (b >= 224) { conv_ch(b - 224, Khi, Klo, xnT, convT, (u16*)smem); return; }
  int m0 = (b / 14) * 128, n0 = (b % 14) * 128;
  f32x16 acc[2][2];
  gemm_core<128, 128>(y2, w1T, D_, m0, n0, (u16*)smem, acc);
  int wave = threadIdx.x >> 6, lane = threadIdx.x & 63;
  int wm = (wave >> 1) * 64, wn = (wave & 1) * 64;
#pragma unroll
  for (int j = 0; j < 2; ++j) {
    int col = n0 + wn + j * 32 + (lane & 31);
    float bb = bf2f(b1[col]);
#pragma unroll
    for (int i = 0; i < 2; ++i)
#pragma unroll
      for (int v = 0; v < 16; ++v) {
        int row = m0 + wm + i * 32 + (v & 3) + 8 * (v >> 2) + 4 * (lane >> 5);
        float z = rbf(rbf(acc[i][j][v]) + bb);
        h[(size_t)row * F_ + col] = f2bf(gelu_t(z));
      }
  }
}

__global__ __launch_bounds__(256) void k3_gemm2(const u16* h, const u16* w2T, const u16* b2, const u16* x,
    const u16* xn, const u16* convT, const u16* Dskip, u16* out) {
  extern __shared__ float smem[];
  int b = blockIdx.x;
  int m0 = (b / 8) * 64, n0 = (b % 8) * 64;
  f32x16 acc[1][1];
  gemm_core<64, 64>(h, w2T, F_, m0, n0, (u16*)smem, acc);
  int wave = threadIdx.x >> 6, lane = threadIdx.x & 63;
  int wm = (wave >> 1) * 32, wn = (wave & 1) * 32;
  int col = n0 + wn + (lane & 31);
  float bb = bf2f(b2[col]), ds = bf2f(Dskip[col]);
#pragma unroll
  for (int v = 0; v < 16; ++v) {
    int row = m0 + wm + (v & 3) + 8 * (v >> 2) + 4 * (lane >> 5);
    float Bv = rbf(rbf(acc[0][0][v]) + bb);
    size_t o = (size_t)row * D_ + col;
    float Av = rbf(bf2f(convT[(size_t)col * L_ + row]) + rbf(bf2f(xn[o]) * ds));
    out[o] = f2bf(rbf(bf2f(x[o]) + Av) + Bv);
  }
}

extern "C" void kernel_launch(void* const* d_in, const int* in_sizes, int n_in,
                              void* d_out, int out_size, void* d_ws, size_t ws_size,
                              hipStream_t stream) {
  const u16* x = (const u16*)d_in[0];
  char* ws = (char*)d_ws;
  const size_t MB = 1 << 20;
  u16* xn = (u16*)(ws); u16* y2 = (u16*)(ws + 2 * MB); u16* xnT = (u16*)(ws + 4 * MB);
  u16* w1T = (u16*)(ws + 6 * MB); u16* w2T = (u16*)(ws + 8 * MB);
  u16* Khi = (u16*)(ws + 10 * MB); u16* Klo = (u16*)(ws + 12 * MB);
  u16* h = (u16*)(ws + 14 * MB); u16* convT = (u16*)(ws + 22 * MB);
  size_t s1 = (41 * AS + 4 * 256 * 16 + 512 + 5 * 256) * 4;
  hipFuncSetAttribute((const void*)k1_prep, hipFuncAttributeMaxDynamicSharedMemorySize, (int)s1);
  k1_prep<<<D_ + LN_BLOCKS + TR_BLOCKS, 256, s1, stream>>>(x, (const u16*)d_in[1], (const u16*)d_in[2],
      (const u16*)d_in[3], (const u16*)d_in[4], (const u16*)d_in[5], (const u16*)d_in[6], (const u16*)d_in[7],
      (const u16*)d_in[10], (const u16*)d_in[12], xn, y2, xnT, w1T, w2T, Khi, Klo);
  size_t s2 = (16 * RLEN + 3072) * 2;
  hipFuncSetAttribute((const void*)k2_gemm1_conv, hipFuncAttributeMaxDynamicSharedMemorySize, (int)s2);
  k2_gemm1_conv<<<224 + D_, 256, s2, stream>>>(y2, w1T, (const u16*)d_in[11], h, Khi, Klo, xnT, convT);
  size_t s3 = 2 * 128 * LDT * 2;
  k3_gemm2<<<256, 256, s3, stream>>>(h, w2T, (const u16*)d_in[13], x, xn, convT, (const u16*)d_in[8], (u16*)d_out);
}
```

```cpp
#include <hip/hip_runtime.h>
#include <hip/hip_bf16.h>
#include <stdint.h>

typedef __attribute__((ext_vector_type(8))) short bf16x8;
typedef __attribute__((ext_vector_type(4))) float f32x4;
typedef __attribute__((ext_vector_type(16))) float f32x16;
typedef unsigned short u16;

#define L_ 2048
#define D_ 512
#define N_ 128
#define F_ 1792

__device__ __forceinline__ float bf2f(u16 v) { return __uint_as_float(((unsigned)v) << 16); }
__device__ __forceinline__ u16 f2bf(float f) {
  unsigned u = __float_as_uint(f);
  u += 0x7FFFu + ((u >> 16) & 1u);
  return (u16)(u >> 16);
}
__device__ __forceinline__ float rbf(float f) { return bf2f(f2bf(f)); }

__device__ __forceinline__ float wsum(float v) {
#pragma unroll
  for (int o = 32; o > 0; o >>= 1) v += __shfl_xor(v, o);
  return v;
}

#define LN_BLOCKS 128
#define TR_BLOCKS 448
__device__ void ln_rows(const u16* __restrict__ x, u16* __restrict__ xn, u16* __restrict__ y2,
                        u16* __restrict__ xnT, int blk, u16* sT) {
  int wave = threadIdx.x >> 6, lane = threadIdx.x & 63;
#pragma unroll
  for (int rr = 0; rr < 4; ++rr) {
    int rloc = wave * 4 + rr;
    int row = blk * 16 + rloc;
    bf16x8 v = *(const bf16x8*)(x + (size_t)row * D_ + lane * 8);
    float f[8];
#pragma unroll
    for (int pass = 0; pass < 2; ++pass) {
      float s = 0.f;
#pragma unroll
      for (int e = 0; e < 8; ++e) { f[e] = bf2f((u16)v[e]); s += f[e]; }
      float mean = wsum(s) * (1.f / D_);
      float q = 0.f;
#pragma unroll
      for (int e = 0; e < 8; ++e) { float dd = f[e] - mean; q += dd * dd; }
      float var = rbf(wsum(q) * (1.f / D_));
      float mb = rbf(mean);
      float den = rbf(sqrtf(rbf(var + 1e-5f)));
#pragma unroll
      for (int e = 0; e < 8; ++e) v[e] = (short)f2bf(rbf(f[e] - mb) / den);
      if (pass == 0) {
        *(bf16x8*)(xn + (size_t)row * D_ + lane * 8) = v;
#pragma unroll
        for (int e = 0; e < 8; ++e) sT[(lane * 8 + e) * 16 + rloc] = (u16)v[e];
      } else {
        *(bf16x8*)(y2 + (size_t)row * D_ + lane * 8) = v;
      }
    }
  }
  __syncthreads();
  for (int idx = threadIdx.x; idx < D_ * 2; idx += 256) {
    int d = idx >> 1, h = idx & 1;
    *(bf16x8*)(xnT + (size_t)d * L_ + blk * 16 + h * 8) = *(const bf16x8*)(sT + d * 16 + h * 8);
  }
}

__device__ void tr_tile(const u16* __restrict__ src, u16* __restrict__ dst, int R, int C, int tr, int tc, u16* s) {
  for (int idx = threadIdx.x; idx < 512; idx += 256) {
    int r = idx >> 3, c8 = idx & 7;
    bf16x8 v = *(const bf16x8*)(src + (size_t)(tr * 64 + r) * C + tc * 64 + c8 * 8);
#pragma unroll
    for (int e = 0; e < 8; ++e) s[(c8 * 8 + e) * 72 + r] = (u16)v[e];
  }
  __syncthreads();
  for (int idx = threadIdx.x; idx < 512; idx += 256) {
    int c = idx >> 3, r8 = idx & 7;
    *(bf16x8*)(dst + (size_t)(tc * 64 + c) * R + tr * 64 + r8 * 8) = *(const bf16x8*)(s + c * 72 + r8 * 8);
  }
}

__device__ __forceinline__ void cexp(float ar, float ai, float l, float& er, float& ei) {
  float m = __expf(ar * l);
  float r = ai * l * 0.15915494309189535f;
  r -= floorf(r);
  er = m * __builtin_amdgcn_cosf(r);
  ei = m * __builtin_amdgcn_sinf(r);
}

#define AS 260
__device__ void kgen(int d, const u16* Are, const u16* Aim, const u16* Bre, const u16* Bim,
                     const u16* Cre, const u16* Cim, const u16* logdt,
                     u16* __restrict__ Khi, u16* __restrict__ Klo, float* sm) {
  float* sA = sm;
  float* sB = sA + 25 * AS;
  float* sP = sB + 2 * 256 * 16;
  float* sK = sP + 512;
  int t = threadIdx.x;
  if (t < N_) {
    float dt = rbf(__expf(bf2f(logdt[d])));
    int o = d * N_ + t;
    float br = dt * bf2f(Bre[o]), bi = dt * bf2f(Bim[o]);
    float cr = bf2f(Cre[o]), ci = bf2f(Cim[o]);
    sP[t] = dt * bf2f(Are[o]);
    sP[128 + t] = dt * bf2f(Aim[o]);
    sP[256 + t] = cr * br - ci * bi;
    sP[384 + t] = cr * bi + ci * br;
  }
  __syncthreads();
  int wave = t >> 6, lane = t & 63;
  int n = t & 127;
  float ar = sP[n], ai = sP[128 + n], cbr = sP[256 + n], cbi = sP[384 + n];
  for (int ph = 0; ph < 2; ++ph) {
    int n0rows = ph == 0 ? 17 : 8;
    int nrows = ph == 0 ? 25 : 17;
    for (int row = (t >> 7); row < nrows; row += 2) {
      int r = 2 * ph + (row >= n0rows ? 1 : 0);
      int j = row >= n0rows ? row - n0rows : row;
      float base = (r == 0) ? 0.f : (float)(128 << r);
      float s = (float)(1 << r);
      float er, ei; cexp(ar, ai, base + 16.f * s * j, er, ei);
      sA[row * AS + n] = cbr * er - cbi * ei;
      sA[row * AS + 128 + n] = -(cbr * ei + cbi * er);
    }
    for (int ri = (t >> 7); ri < 32; ri += 2) {
      int rl = ri >> 4, i = ri & 15;
      float er, ei; cexp(ar, ai, (float)((1 << (2 * ph + rl)) * i), er, ei);
      sB[(rl * 256 + n) * 16 + i] = er;
      sB[(rl * 256 + 128 + n) * 16 + i] = ei;
    }
    __syncthreads();
    int ntile = ph == 0 ? 3 : 2;
    if (wave < ntile) {
      int rl, row0, nrow, slot;
      if (ph == 0) { rl = wave == 2; row0 = wave == 0 ? 0 : (wave == 1 ? 16 : 17); nrow = wave == 0 ? 16 : (wave == 1 ? 1 : 8); slot = wave; }
      else { rl = wave; row0 = wave == 0 ? 0 : 8; nrow = wave == 0 ? 8 : 9; slot = 3 + wave; }
      int rr = lane & 15; if (rr >= nrow) rr = 0;
      const float* pa = sA + (row0 + rr) * AS + (lane >> 4);
      const float* pb = sB + rl * 4096 + (lane >> 4) * 16 + (lane & 15);
      f32x4 acc = {0, 0, 0, 0}, acc2 = {0, 0, 0, 0}, acc3 = {0, 0, 0, 0}, acc4 = {0, 0, 0, 0};
#pragma unroll 4
      for (int kk = 0; kk < 64; kk += 4) {
        acc = __builtin_amdgcn_mfma_f32_16x16x4f32(pa[kk * 4], pb[kk * 64], acc, 0, 0, 0);
        acc2 = __builtin_amdgcn_mfma_f32_16x16x4f32(pa[kk * 4 + 4], pb[kk * 64 + 64], acc2, 0, 0, 0);
        acc3 = __builtin_amdgcn_mfma_f32_16x16x4f32(pa[kk * 4 + 8], pb[kk * 64 + 128], acc3, 0, 0, 0);
        acc4 = __builtin_amdgcn_mfma_f32_16x16x4f32(pa[kk * 4 + 12], pb[kk * 64 + 192], acc4, 0, 0, 0);
      }
      acc += acc2 + acc3 + acc4;
#pragma unroll
      for (int v = 0; v < 4; ++v) sK[slot * 256 + ((lane >> 4) * 4 + v) * 16 + (lane & 15)] = acc[v];
    }
    __syncthreads();
  }
  {
    int l0 = t * 8;
    bf16x8 oh, ol;
#pragma unroll
    for (int e = 0; e < 8; ++e) {
      int lb = (int)bf2f(f2bf((float)(l0 + e)));
      int idx;
      if (lb <= 256) idx = lb;
      else {
        int r = (lb < 512) ? 1 : (lb < 1024 ? 2 : 3);
        idx = 512 + (r - 1) * 256 + ((lb - (128 << r)) >> r);
      }
      float k = sK[idx];
      u16 hi = f2bf(k);
      oh[e] = (short)hi; ol[e] = (short)f2bf(k - bf2f(hi));
    }
    *(bf16x8*)(Khi + (size_t)d * L_ + l0) = oh;
    *(bf16x8*)(Klo + (size_t)d * L_ + l0) = ol;
  }
}

__global__ __launch_bounds__(256) void k1_prep(const u16* x, const u16* Are, const u16* Aim, const u16* Bre,
    const u16* Bim, const u16* Cre, const u16* Cim, const u16* logdt, const u16* w1, const u16* w2,
    u16* xn, u16* y2, u16* xnT, u16* w1T, u16* w2T, u16* Khi, u16* Klo) {
  extern __shared__ float smem[];
  int b = blockIdx.x;
  if (b < LN_BLOCKS) { ln_rows(x, xn, y2, xnT, b, (u16*)smem); return; }
  b -= LN_BLOCKS;
  if (b < 224) { tr_tile(w1, w1T, D_, F_, b / 28, b % 28, (u16*)smem); return; }
  b -= 224;
  if (b < 224) { tr_tile(w2, w2T, F_, D_, b / 8, b % 8, (u16*)smem); return; }
  b -= 224;
  kgen(b, Are, Aim, Bre, Bim, Cre, Cim, logdt, Khi, Klo, smem);
}

#define LDT 40
template <int BM, int BN>
__device__ __forceinline__ void gemm_core(const u16* __restrict__ A, const u16* __restrict__ Bt, int K,
                                          int m0, int n0, u16* sm, f32x16 (&acc)[BM / 64][BN / 64]) {
  constexpr int TM = BM / 64, TN = BN / 64;
  int t = threadIdx.x, wave = t >> 6, lane = t & 63;
  int wm = (wave >> 1) * (BM / 2), wn = (wave & 1) * (BN / 2);
  u16* sA = sm; u16* sB = sm + 2 * BM * LDT;
#pragma unroll
  for (int i = 0; i < TM; ++i)
#pragma unroll
    for (int j = 0; j < TN; ++j)
#pragma unroll
      for (int v = 0; v < 16; ++v) acc[i][j][v] = 0.f;
  constexpr int NA = BM * 4 / 256, NB = BN * 4 / 256;
  bf16x8 ra[NA], rb[NB];
  auto gload = [&](int k0) {
#pragma unroll
    for (int i = 0; i < NA; ++i) { int c = t + i * 256; ra[i] = *(const bf16x8*)(A + (size_t)(m0 + (c >> 2)) * K + k0 + (c & 3) * 8); }
#pragma unroll
    for (int i = 0; i < NB; ++i) { int c = t + i * 256; rb[i] = *(const bf16x8*)(Bt + (size_t)(n0 + (c >> 2)) * K + k0 + (c & 3) * 8); }
  };
  auto swrite = [&](int buf) {
#pragma unroll
    for (int i = 0; i < NA; ++i) { int c = t + i * 256; *(bf16x8*)(sA + buf * BM * LDT + (c >> 2) * LDT + (c & 3) * 8) = ra[i]; }
#pragma unroll
    for (int i = 0; i < NB; ++i) { int c = t + i * 256; *(bf16x8*)(sB + buf * BN * LDT + (c >> 2) * LDT + (c & 3) * 8) = rb[i]; }
  };
  gload(0); swrite(0); __syncthreads();
  int nk = K / 32;
  for (int kt = 0; kt < nk; ++kt) {
    int buf = kt & 1;
    if (kt + 1 < nk) gload((kt + 1) * 32);
#pragma unroll
    for (int ks = 0; ks < 2; ++ks) {
      bf16x8 fa[TM], fb[TN];
#pragma unroll
      for (int i = 0; i < TM; ++i) fa[i] = *(const bf16x8*)(sA + buf * BM * LDT + (wm + i * 32 + (lane & 31)) * LDT + ks * 16 + (lane >> 5) * 8);
#pragma unroll
      for (int j = 0; j < TN; ++j) fb[j] = *(const bf16x8*)(sB + buf * BN * LDT + (wn + j * 32 + (lane & 31)) * LDT + ks * 16 + (lane >> 5) * 8);
#pragma unroll
      for (int i = 0; i < TM; ++i)
#pragma unroll
        for (int j = 0; j < TN; ++j) acc[i][j] = __builtin_amdgcn_mfma_f32_32x32x16_bf16(fa[i], fb[j], acc[i][j], 0, 0, 0);
    }
    if (kt + 1 < nk) swrite(buf ^ 1);
    __syncthreads();
  }
}

__device__ __forceinline__ float gelu_t(float x) {
  float u = 0.7978845608028654f * (x + 0.044715f * x * x * x);
  return 0.5f * x * (1.f + tanhf(u));
}

#define RLEN 2112
__device__ void conv_ch(int d, const u16* __restrict__ Khi, const u16* __restrict__ Klo,
                        const u16* __restrict__ xnT, u16* __restrict__ convT, u16* sm) {
  u16* sR = sm;
  u16* sU = sm + 16 * RLEN;
  float* sRed = (float*)sR;
  int t = threadIdx.x, wave = t >> 6, lane = t & 63;
  {
    bf16x8 vh = *(const bf16x8*)(Khi + (size_t)d * L_ + t * 8);
    bf16x8 vl = *(const bf16x8*)(Klo + (size_t)d * L_ + t * 8);
    bf16x8 rh, rl;
#pragma unroll
    for (int e = 0; e < 8; ++e) { rh[e] = vh[7 - e]; rl[e] = vl[7 - e]; }
    *(bf16x8*)(sR + (L_ - 8 - t * 8)) = rh;
    *(bf16x8*)(sR + 8 * RLEN + (L_ - 8 - t * 8)) = rl;
    if (t < 8) {
      *(bf16x8*)(sR + L_ + t * 8) = (bf16x8){0, 0, 0, 0, 0, 0, 0, 0};
      *(bf16x8*)(sR + 8 * RLEN + L_ + t * 8) = (bf16x8){0, 0, 0, 0, 0, 0, 0, 0};
    }
    __syncthreads();
    for (int y8 = t; y8 < RLEN / 8 - 1; y8 += 256) {
#pragma unroll
      for (int hl = 0; hl < 2; ++hl) {
        bf16x8 a0 = *(const bf16x8*)(sR + hl * 8 * RLEN + y8 * 8);
        bf16x8 a1 = *(const bf16x8*)(sR + hl * 8 * RLEN + y8 * 8 + 8);
#pragma unroll
        for (int c = 1; c < 8; ++c) {
          bf16x8 o;
#pragma unroll
          for (int e = 0; e < 8; ++e) o[e] = (e + c < 8) ? a0[e + c] : a1[e + c - 8];
          *(bf16x8*)(sR + (hl * 8 + c) * RLEN + y8 * 8) = o;
        }
      }
    }
  }
  for (int i = t; i < 128; i += 256) *(bf16x8*)(sU + i * 8) = (bf16x8){0, 0, 0, 0, 0, 0, 0, 0};
  *(bf16x8*)(sU + 1024 + t * 8) = *(const bf16x8*)(xnT + (size_t)d * L_ + t * 8);
  __syncthreads();
  int q16 = lane & 15, g = lane >> 4;
  int c = (7 - q16) & 7;
  const u16* pR = sR + c * RLEN + (2047 - q16 + 8 * g - c);
  f32x4 acc[4][2];
#pragma unroll
  for (int a = 0; a < 4; ++a) { acc[a][0] = (f32x4){0, 0, 0, 0}; acc[a][1] = (f32x4){0, 0, 0, 0}; }
  for (int m = wave; m < 32; m += 4) {
#pragma unroll
    for (int sc = 0; sc < 2; ++sc) {
      bf16x8 b1 = *(const bf16x8*)(sU + 1024 + 64 * (16 + q16 - m) + 32 * sc + 8 * g);
      bf16x8 b0 = (bf16x8){0, 0, 0, 0, 0, 0, 0, 0};
      if (m < 16) b0 = *(const bf16x8*)(sU + 1024 + 64 * (q16 - m) + 32 * sc + 8 * g);
#pragma unroll
      for (int qt = 0; qt < 4; ++qt) {
        int tt = 4 * m + qt - 2 * sc;
        bf16x8 ah = *(const bf16x8*)(pR - 16 * tt);
        bf16x8 al = *(const bf16x8*)(pR + 8 * RLEN - 16 * tt);
        acc[qt][1] = __builtin_amdgcn_mfma_f32_16x16x32_bf16(ah, b1, acc[qt][1], 0, 0, 0);
        acc[qt][1] = __builtin_amdgcn_mfma_f32_16x16x32_bf16(al, b1, acc[qt][1], 0, 0, 0);
        if (m < 16) {
          acc[qt][0] = __builtin_amdgcn_mfma_f32_16x16x32_bf16(ah, b0, acc[qt][0], 0, 0, 0);
          acc[qt][0] = __builtin_amdgcn_mfma_f32_16x16x32_bf16(al, b0, acc[qt][0], 0, 0, 0);
        }
      }
    }
  }
  __syncthreads();
#pragma unroll
  for (int qt = 0; qt < 4; ++qt)
#pragma unroll
    for (int pt = 0; pt < 2; ++pt)
#pragma unroll
      for (int v = 0; v < 4; ++v)
        sRed[wave * L_ + 64 * (pt * 16 + q16) + 16 * qt + 4 * g + v] = acc[qt][pt][v];
  __syncthreads();
  for (int l = t * 8; l < L_; l += 2048) {
    bf16x8 o;
#pragma unroll
    for (int e = 0; e < 8; ++e)
      o[e] = (short)f2bf(sRed[l + e] + sRed[L_ + l + e] + sRed[2 * L_ + l + e] + sRed[3 * L_ + l + e]);
    *(bf16x8*)(convT + (size_t)d * L_ + l) = o;
  }
}

__global__ __launch_bounds__(256) void k2_gemm1_conv(const u16* y2, const u16* w1T, const u16* b1, u16* h,
    const u16* Khi, const u16* Klo, const u16* xnT, u16* convT) {
  extern __shared__ float smem[];
  int b = blockIdx.x;
  if (b >= 224) { conv_ch(b - 224, Khi, Klo, xnT, convT, (u16*)smem); return; }
  int m0 = (b / 14) * 128, n0 = (b % 14) * 128;
  f32x16 acc[2][2];
  gemm_core<128, 128>(y2, w1T, D_, m0, n0, (u16*)smem, acc);
  int wave = threadIdx.x >> 6, lane = threadIdx.x & 63;
  int wm = (wave >> 1) * 64, wn = (wave & 1) * 64;
#pragma unroll
  for (int j = 0; j < 2; ++j) {
    int col = n0 + wn + j * 32 + (lane & 31);
    float bb = bf2f(b1[col]);
#pragma unroll
    for (int i = 0; i < 2; ++i)
#pragma unroll
      for (int v = 0; v < 16; ++v) {
        int row = m0 + wm + i * 32 + (v & 3) + 8 * (v >> 2) + 4 * (lane >> 5);
        float z = rbf(rbf(acc[i][j][v]) + bb);
        h[(size_t)row * F_ + col] = f2bf(gelu_t(z));
      }
  }
}

__global__ __launch_bounds__(256) void k3_gemm2(const u16* h, const u16* w2T, const u16* b2, const u16* x,
    const u16* xn, const u16* convT, const u16* Dskip, u16* out) {
  extern __shared__ float smem[];
  int b = blockIdx.x;
  int m0 = (b / 8) * 64, n0 = (b % 8) * 64;
  f32x16 acc[1][1];
  gemm_core<64, 64>(h, w2T, F_, m0, n0, (u16*)smem, acc);
  int wave = threadIdx.x >> 6, lane = threadIdx.x & 63;
  int wm = (wave >> 1) * 32, wn = (wave & 1) * 32;
  int col = n0 + wn + (lane & 31);
  float bb = bf2f(b2[col]), ds = bf2f(Dskip[col]);
#pragma unroll
  for (int v = 0; v < 16; ++v) {
    int row = m0 + wm + (v & 3) + 8 * (v >> 2) + 4 * (lane >> 5);
    float Bv = rbf(rbf(acc[0][0][v]) + bb);
    size_t o = (size_t)row * D_ + col;
    float Av = rbf(bf2f(convT[(size_t)col * L_ + row]) + rbf(bf2f(xn[o]) * ds));
    out[o] = f2bf(rbf(bf2f(x[o]) + Av) + Bv);
  }
}

extern "C" void kernel_launch(void* const* d_in, const int* in_sizes, int n_in,
                              void* d_out, int out_size, void* d_ws, size_t ws_size,
                              hipStream_t stream) {
  const u16* x = (const u16*)d_in[0];
  char* ws = (char*)d_ws;
  const size_t MB = 1 << 20;
  u16* xn = (u16*)(ws); u16* y2 = (u16*)(ws + 2 * MB); u16* xnT = (u16*)(ws + 4 * MB);
  u16* w1T = (u16*)(ws + 6 * MB); u16* w2T = (u16*)(ws + 8 * MB);
  u16* Khi = (u16*)(ws + 10 * MB); u16* Klo = (u16*)(ws + 12 * MB);
  u16* h = (u16*)(ws + 14 * MB); u16* convT = (u16*)(ws + 22 * MB);
  size_t s1 = (25 * AS + 2 * 256 * 16 + 512 + 6 * 256) * 4;
  hipFuncSetAttribute((const void*)k1_prep, hipFuncAttributeMaxDynamicSharedMemorySize, (int)s1);
  k1_prep<<<D_ + LN_BLOCKS + TR_BLOCKS, 256, s1, stream>>>(x, (const u16*)d_in[1], (const u16*)d_in[2],
      (const u16*)d_in[3], (const u16*)d_in[4], (const u16*)d_in[5], (const u16*)d_in[6], (const u16*)d_in[7],
      (const u16*)d_in[10], (const u16*)d_in[12], xn, y2, xnT, w1T, w2T, Khi, Klo);
  size_t s2 = (16 * RLEN + 3072) * 2;
  hipFuncSetAttribute((const void*)k2_gemm1_conv, hipFuncAttributeMaxDynamicSharedMemorySize, (int)s2);
  k2_gemm1_conv<<<224 + D_, 256, s2, stream>>>(y2, w1T, (const u16*)d_in[11], h, Khi, Klo, xnT, convT);
  size_t s3 = 2 * 128 * LDT * 2;
  k3_gemm2<<<256, 256, s3, stream>>>(h, w2T, (const u16*)d_in[13], x, xn, convT, (const u16*)d_in[8], (u16*)d_out);
}
```
